```python
import torch

def _softmax(x, axis=-1):
    return torch.softmax(x, dim=axis)
def _gelu(x):
    return torch.nn.functional.gelu(x, approximate="tanh")
def _silu(x):
    return torch.nn.functional.silu(x)
def _sigmoid(x):
    return torch.sigmoid(x)
def _layernorm(x, axis=-1, eps=1e-5):
    m = x.mean(dim=axis, keepdim=True)
    v = x.var(dim=axis, keepdim=True, unbiased=False)
    return (x - m) / torch.sqrt(v + eps)
def _scatter_add(tgt, idx, src, axis=0):
    return tgt.clone().index_add_(axis, idx, src)
def _segment_sum(src, seg, n):
    out = torch.zeros((n,) + src.shape[1:], dtype=src.dtype)
    return out.index_add_(0, seg, src)
def _pad(x, pw):
    flat = tuple(v for pair in reversed(pw) for v in pair)
    return torch.nn.functional.pad(x, flat)

kernel_name = 'parallel_sum__softmax_full_mha__moe_arch_mod__prompt'

def setup_inputs():

    _w = (lambda a: (lambda t: t.to(torch.bfloat16) if t.is_floating_point() else t)(torch.as_tensor(a)))
    def _A_setup():
        B, S, D = 1, 2048, 512
        return {
            "x": _w((lambda *s: torch.randn(*s, dtype=torch.bfloat16) * 0.05)(B, S, D)),
            "wqkv": _w((lambda *s: torch.randn(*s, dtype=torch.bfloat16) * 0.2)(D, 3 * D)),
            "wo": _w((lambda *s: torch.randn(*s, dtype=torch.bfloat16) * 0.2)(D, D)),
        }
    _A_in = _A_setup()

    def _B_setup():
        B, S, D, F, C = 1, 2048, 512, 1376, 8
        return {
            "x": _w((lambda *s: torch.randn(*s, dtype=torch.bfloat16) * 0.05)(B, S, D)),
            "w_r": _w((lambda *s: torch.randn(*s, dtype=torch.bfloat16) * 1.6)(D)),
            "w_gate": _w((lambda *s: torch.randn(*s, dtype=torch.bfloat16) * 0.2)(D, F)),
            "w_up": _w((lambda *s: torch.randn(*s, dtype=torch.bfloat16) * 0.2)(D, F)),
            "w_down": _w((lambda *s: torch.randn(*s, dtype=torch.bfloat16) * 0.2)(F, D)),
        }
    _B_in = _B_setup()

    return {
        "A_x": _A_in["x"],
        "A_wqkv": _A_in["wqkv"],
        "A_wo": _A_in["wo"],
        "B_x": _B_in["x"],
        "B_w_r": _B_in["w_r"],
        "B_w_gate": _B_in["w_gate"],
        "B_w_up": _B_in["w_up"],
        "B_w_down": _B_in["w_down"],
    }

def reference(A_x, A_wqkv, A_wo, B_x, B_w_r, B_w_gate, B_w_up, B_w_down):
    def _A_ref(x, wqkv, wo):
        B, S, D = x.shape
        H = 32; Dh = D // H
        qkv = (lambda x, *d: x.permute(*d))((x @ wqkv).reshape(B, S, 3, H, Dh), 2, 0, 3, 1, 4)
        q, k, v = qkv[0], qkv[1], qkv[2]
        scale = Dh ** -0.5
        scores = torch.einsum("bhqd,bhkd->bhqk", q, k) * scale
        p = _softmax(scores, axis=-1)
        o = torch.einsum("bhqk,bhkd->bhqd", p, v)
        return (lambda x, *d: x.permute(*d))(o, 0, 2, 1, 3).reshape(B, S, D) @ wo

    def _B_ref(x, w_r, w_gate, w_up, w_down):
        B, S, D = x.shape
        C = 8
        r = x @ w_r
        idx = torch.argsort(-r, axis=-1)[:, :C]
        idx_e = (lambda x, n, axis: torch.repeat_interleave(x, n, dim=axis))(idx.reshape(B, C, 1), D, axis=2)
        x_sel = (lambda x, i, axis: torch.take_along_dim(x, i, dim=axis))(x, idx_e, axis=1)
        g = _silu(x_sel @ w_gate)
        u = x_sel @ w_up
        y = (g * u) @ w_down
        r_sel = _sigmoid((lambda x, i, axis: torch.take_along_dim(x, i, dim=axis))(r, idx, axis=1))
        y = y * r_sel.reshape(B, C, 1)
        b_off = (torch.arange(B) * S).reshape(B, 1)
        flat_idx = (idx + b_off).reshape(B * C)
        zero = (x * 0.0).reshape(B * S, D)
        upd = _scatter_add(zero, flat_idx, y.reshape(B * C, D), axis=0)
        return x + upd.reshape(B, S, D)

    x = A_x
    xn = _layernorm(x, axis=-1)
    return x + _A_ref(xn, A_wqkv, A_wo) + _B_ref(xn, B_w_r, B_w_gate, B_w_up, B_w_down)
```

```cpp
#include <hip/hip_runtime.h>
#include <hip/hip_bf16.h>
#include <stdint.h>


typedef unsigned short u16;
typedef __attribute__((ext_vector_type(8))) short bf16x8;
typedef __attribute__((ext_vector_type(4))) short s16x4;
typedef __attribute__((ext_vector_type(16))) float f32x16;
typedef __attribute__((ext_vector_type(4))) unsigned u32x4;
typedef __attribute__((ext_vector_type(2))) unsigned u32x2;
typedef __attribute__((address_space(3))) s16x4 lds_s16x4;

#define DI __device__ __forceinline__
#define MFMA32(a, b, c) __builtin_amdgcn_mfma_f32_32x32x16_bf16((a), (b), (c), 0, 0, 0)

constexpr int S_ = 2048, D_ = 512, F_ = 1376, NH = 32;
constexpr int NMOE = 43;

DI float bf2f(u16 v) { return __uint_as_float(((unsigned)v) << 16); }
DI u16 f2bf(float f) { __bf16 b = (__bf16)f; return __builtin_bit_cast(u16, b); }
DI float rbf(float f) { return bf2f(f2bf(f)); }
DI float lo16(unsigned w) { return __uint_as_float(w << 16); }
DI float hi16(unsigned w) { return __uint_as_float(w & 0xffff0000u); }
DI unsigned pk2(float lo, float hi) { return (unsigned)f2bf(lo) | ((unsigned)f2bf(hi) << 16); }

DI bf16x8 tr_frag(const u16* lds_lane_base, int elem_off, int row_stride) {
  s16x4 a = __builtin_amdgcn_ds_read_tr16_b64_v4i16((lds_s16x4*)(lds_lane_base + elem_off));
  s16x4 b = __builtin_amdgcn_ds_read_tr16_b64_v4i16((lds_s16x4*)(lds_lane_base + elem_off + 4 * row_stride));
  return __builtin_shufflevector(a, b, 0, 1, 2, 3, 4, 5, 6, 7);
}

__global__ __launch_bounds__(256) void k1_ln_qkv(const u16* __restrict__ x, const u16* __restrict__ wqkv,
                                                 const u16* __restrict__ w_r, u16* __restrict__ xn_out,
                                                 float* __restrict__ r_out, u16* __restrict__ Qo,
                                                 u16* __restrict__ Ko, u16* __restrict__ Vt) {
  __shared__ __attribute__((aligned(16))) u16 wl[512 * 96];
  const int tid = threadIdx.x, wave = tid >> 6, lane = tid & 63;
  const int r = lane & 31, h = lane >> 5;
  const int b = blockIdx.x;
  const int nt = (b & 7) | (((b >> 3) & 1) << 3);
  const int mt = b >> 4;

#pragma unroll
  for (int i = 0; i < 24; ++i) {
    const int piece = (i * 4 + wave) * 64 + lane;
    const int k = piece / 12, c = (piece % 12) * 8;
    const u16* src = wqkv + (size_t)k * 1536 + nt * 96 + c;
    __builtin_amdgcn_global_load_lds((const unsigned*)src, (unsigned*)(wl + (i * 4 + wave) * 512), 16, 0, 0);
  }

  const int row = mt * 128 + wave * 32 + r;
  const u16* xr = x + (size_t)row * 512 + 8 * h;
  u32x4 xa[32];
#pragma unroll
  for (int t = 0; t < 32; ++t) xa[t] = *(const u32x4*)(xr + 16 * t);

  float s = 0.f;
#pragma unroll
  for (int t = 0; t < 32; ++t)
#pragma unroll
    for (int j = 0; j < 4; ++j) s += lo16(xa[t][j]) + hi16(xa[t][j]);
  s += __shfl_xor(s, 32);
  const float mean = s * (1.0f / 512.0f);
  float vs = 0.f;
#pragma unroll
  for (int t = 0; t < 32; ++t)
#pragma unroll
    for (int j = 0; j < 4; ++j) {
      float d0 = lo16(xa[t][j]) - mean, d1 = hi16(xa[t][j]) - mean;
      vs += d0 * d0 + d1 * d1;
    }
  vs += __shfl_xor(vs, 32);
  const float m_b = rbf(mean), v_b = rbf(vs * (1.0f / 512.0f));
  const float den = rbf(sqrtf(rbf(v_b + 1e-5f)));
  const float inv = 1.0f / den;
#pragma unroll
  for (int t = 0; t < 32; ++t)
#pragma unroll
    for (int j = 0; j < 4; ++j) {
      float a0 = rbf(lo16(xa[t][j]) - m_b) * inv, a1 = rbf(hi16(xa[t][j]) - m_b) * inv;
      xa[t][j] = pk2(a0, a1);
    }

  if (nt == 0) {
    u16* xo = xn_out + (size_t)row * 512 + 8 * h;
    float rs = 0.f;
#pragma unroll
    for (int t = 0; t < 32; ++t) {
      *(u32x4*)(xo + 16 * t) = xa[t];
      u32x4 wv = *(const u32x4*)(w_r + 16 * t + 8 * h);
#pragma unroll
      for (int j = 0; j < 4; ++j) rs += lo16(xa[t][j]) * lo16(wv[j]) + hi16(xa[t][j]) * hi16(wv[j]);
    }
    rs += __shfl_xor(rs, 32);
    if (h == 0) r_out[row] = rbf(rs);
  }

  asm volatile("s_waitcnt vmcnt(0)" ::: "memory");
  __syncthreads();

  const int g = lane >> 4, i16 = lane & 15;
  const u16* lb = wl + (8 * (g >> 1) + (i16 >> 2)) * 96 + 16 * (g & 1) + 4 * (i16 & 3);
  f32x16 acc[3];
#pragma unroll
  for (int c = 0; c < 3; ++c)
#pragma unroll
    for (int i = 0; i < 16; ++i) acc[c][i] = 0.f;
  bool isv[3];
#pragma unroll
  for (int c = 0; c < 3; ++c) isv[c] = (nt * 96 + c * 32) >= 1024;

#pragma unroll
  for (int t = 0; t < 32; ++t) {
    const bf16x8 xf = __builtin_bit_cast(bf16x8, xa[t]);
#pragma unroll
    for (int c = 0; c < 3; ++c) {
      bf16x8 wf = tr_frag(lb, 16 * t * 96 + c * 32, 96);
      if (isv[c]) acc[c] = MFMA32(xf, wf, acc[c]);
      else acc[c] = MFMA32(wf, xf, acc[c]);
    }
  }

  const int tok0 = mt * 128 + wave * 32;
#pragma unroll
  for (int c = 0; c < 3; ++c) {
    const int col0 = nt * 96 + c * 32;
    const int sec = col0 >> 9, hp = (col0 & 511) >> 4;
    if (sec < 2) {
      u16* dst = sec == 0 ? Qo : Ko;
#pragma unroll
      for (int gq = 0; gq < 4; ++gq) {
        const int head = hp + (gq >> 1), d = 8 * (gq & 1) + 4 * h;
        u32x2 v = {pk2(acc[c][4 * gq], acc[c][4 * gq + 1]), pk2(acc[c][4 * gq + 2], acc[c][4 * gq + 3])};
        *(u32x2*)(dst + ((size_t)(head * 2048 + tok0 + r) * 16 + d)) = v;
      }
    } else {
      const int head = hp + (r >> 4), d = r & 15;
#pragma unroll
      for (int gq = 0; gq < 4; ++gq) {
        u32x2 v = {pk2(acc[c][4 * gq], acc[c][4 * gq + 1]), pk2(acc[c][4 * gq + 2], acc[c][4 * gq + 3])};
        *(u32x2*)(Vt + ((size_t)(head * 16 + d) * 2048 + tok0 + 8 * gq + 4 * h)) = v;
      }
    }
  }
}

DI void moe_block(int mb, const float* __restrict__ r_in, const u16* __restrict__ xn, const u16* __restrict__ wg,
                  const u16* __restrict__ wu, const u16* __restrict__ wd, float* __restrict__ ypart,
                  int* __restrict__ idx_out, float* __restrict__ rsel_out) {
  __shared__ float xs[8][512];
  __shared__ float gu[2][8][32];
  __shared__ float hs[8][32];
  __shared__ float redv[8];
  __shared__ int redi[8];
  __shared__ int sidx[8];
  const int tid = threadIdx.x, wave = tid >> 6, lane = tid & 63;
  float v[4];
#pragma unroll
  for (int j = 0; j < 4; ++j) v[j] = r_in[tid * 4 + j];
  for (int it = 0; it < 8; ++it) {
    float bv = v[0];
    int bi = tid * 4;
#pragma unroll
    for (int j = 1; j < 4; ++j)
      if (v[j] > bv) { bv = v[j]; bi = tid * 4 + j; }
#pragma unroll
    for (int o = 32; o > 0; o >>= 1) {
      float ov = __shfl_xor(bv, o);
      int oi = __shfl_xor(bi, o);
      if (ov > bv || (ov == bv && oi < bi)) { bv = ov; bi = oi; }
    }
    if (lane == 0) { redv[wave] = bv; redi[wave] = bi; }
    __syncthreads();
    float fv = redv[0];
    int fi = redi[0];
#pragma unroll
    for (int w = 1; w < 8; ++w)
      if (redv[w] > fv || (redv[w] == fv && redi[w] < fi)) { fv = redv[w]; fi = redi[w]; }
    if (tid == 0) {
      sidx[it] = fi;
      if (mb == 0) { idx_out[it] = fi; rsel_out[it] = rbf(1.0f / (1.0f + __expf(-fv))); }
    }
#pragma unroll
    for (int j = 0; j < 4; ++j)
      if (tid * 4 + j == fi) v[j] = -INFINITY;
    __syncthreads();
  }
#pragma unroll
  for (int tk = 0; tk < 8; ++tk) xs[tk][tid] = bf2f(xn[(size_t)sidx[tk] * 512 + tid]);
  __syncthreads();
  const int f0 = mb * 32;
  {
    const int mat = tid >> 8, tok = (tid >> 5) & 7, col = tid & 31;
    const u16* w = (mat ? wu : wg) + f0 + col;
    float acc = 0.f;
#pragma unroll 8
    for (int k = 0; k < 512; ++k) acc += xs[tok][k] * bf2f(w[(size_t)k * F_]);
    gu[mat][tok][col] = acc;
  }
  __syncthreads();
  if (tid < 256) {
    const int tok = tid >> 5, col = tid & 31;
    float gg = rbf(gu[0][tok][col]);
    float g = rbf(gg / (1.0f + __expf(-gg)));
    float u = rbf(gu[1][tok][col]);
    hs[tok][col] = rbf(g * u);
  }
  __syncthreads();
  float acc[8];
#pragma unroll
  for (int tk = 0; tk < 8; ++tk) acc[tk] = 0.f;
#pragma unroll 4
  for (int f = 0; f < 32; ++f) {
    float w = bf2f(wd[(size_t)(f0 + f) * 512 + tid]);
#pragma unroll
    for (int tk = 0; tk < 8; ++tk) acc[tk] += hs[tk][f] * w;
  }
#pragma unroll
  for (int tk = 0; tk < 8; ++tk) ypart[((size_t)mb * 8 + tk) * 512 + tid] = acc[tk];
}

__global__ __launch_bounds__(512) void k2_attn(const u16* __restrict__ Q, const u16* __restrict__ K,
                                               const u16* __restrict__ Vt, u16* __restrict__ O,
                                               const float* __restrict__ r_in, const u16* __restrict__ xn,
                                               const u16* __restrict__ wg, const u16* __restrict__ wu,
                                               const u16* __restrict__ wd, float* __restrict__ ypart,
                                               int* __restrict__ idx_out, float* __restrict__ rsel_out) {
  int b = blockIdx.x;
  if (b < NMOE) {
    moe_block(b, r_in, xn, wg, wu, wd, ypart, idx_out, rsel_out);
    return;
  }
  b -= NMOE;
  const int tid = threadIdx.x, wave = tid >> 6, lane = tid & 63;
  const int r = lane & 31, h = lane >> 5;
  const int head = (b & 7) * 4 + ((b >> 3) & 3);
  const int qb = b >> 5;
  const int q0 = qb * 256 + wave * 32;

  const bf16x8 qf = *(const bf16x8*)(Q + ((size_t)(head * 2048 + q0 + r) * 16 + 8 * h));
  const int pir = (r & 0x13) | ((r & 4) << 1) | ((r & 8) >> 1);
  const u16* kp = K + ((size_t)(head * 2048 + pir) * 16 + 8 * h);
  const u16* vp = Vt + ((size_t)(head * 16 + (r & 15)) * 2048 + 8 * h);
  const short one = (short)0x3f80;
  const short vc = (r == 16) ? one : (short)0;
  const bf16x8 vconst = {vc, vc, vc, vc, vc, vc, vc, vc};

  constexpr float C = 0.25f * 1.4426950408889634f;
  float m = -INFINITY;
  f32x16 o;
#pragma unroll
  for (int i = 0; i < 16; ++i) o[i] = 0.f;
  f32x16 zero;
#pragma unroll
  for (int i = 0; i < 16; ++i) zero[i] = 0.f;

  for (int kt = 0; kt < 32; ++kt) {
    const bf16x8 k0 = *(const bf16x8*)(kp + (size_t)kt * 1024);
    const bf16x8 k1 = *(const bf16x8*)(kp + (size_t)kt * 1024 + 512);
    bf16x8 v0 = vconst, v1 = vconst, v2 = vconst, v3 = vconst;
    if (r < 16) {
      v0 = *(const bf16x8*)(vp + kt * 64);
      v1 = *(const bf16x8*)(vp + kt * 64 + 16);
      v2 = *(const bf16x8*)(vp + kt * 64 + 32);
      v3 = *(const bf16x8*)(vp + kt * 64 + 48);
    }
    f32x16 s0 = MFMA32(k0, qf, zero);
    f32x16 s1 = MFMA32(k1, qf, zero);
    float tmax = fmaxf(s0[0], s1[0]);
#pragma unroll
    for (int i = 1; i < 16; ++i) tmax = fmaxf(tmax, fmaxf(s0[i], s1[i]));
    tmax = fmaxf(tmax, __shfl_xor(tmax, 32));
    if (__any(tmax > m)) {
      const float mn = fmaxf(m, tmax);
      const float alpha = __builtin_amdgcn_exp2f((m - mn) * C);
#pragma unroll
      for (int i = 0; i < 9; ++i) o[i] *= alpha;
      m = mn;
    }
    const float mc = -m * C;
#pragma unroll
    for (int i = 0; i < 16; ++i) {
      s0[i] = __builtin_amdgcn_exp2f(fmaf(s0[i], C, mc));
      s1[i] = __builtin_amdgcn_exp2f(fmaf(s1[i], C, mc));
    }
    u32x4 p0 = {pk2(s0[0], s0[1]), pk2(s0[2], s0[3]), pk2(s0[4], s0[5]), pk2(s0[6], s0[7])};
    u32x4 p1 = {pk2(s0[8], s0[9]), pk2(s0[10], s0[11]), pk2(s0[12], s0[13]), pk2(s0[14], s0[15])};
    u32x4 p2 = {pk2(s1[0], s1[1]), pk2(s1[2], s1[3]), pk2(s1[4], s1[5]), pk2(s1[6], s1[7])};
    u32x4 p3 = {pk2(s1[8], s1[9]), pk2(s1[10], s1[11]), pk2(s1[12], s1[13]), pk2(s1[14], s1[15])};
    o = MFMA32(v0, __builtin_bit_cast(bf16x8, p0), o);
    o = MFMA32(v1, __builtin_bit_cast(bf16x8, p1), o);
    o = MFMA32(v2, __builtin_bit_cast(bf16x8, p2), o);
    o = MFMA32(v3, __builtin_bit_cast(bf16x8, p3), o);
  }
  const float l = __shfl(o[8], r);
  const float inv = 1.0f / l;
  u16* op = O + (size_t)(q0 + r) * 512 + head * 16 + 4 * h;
  u32x2 w0 = {pk2(o[0] * inv, o[1] * inv), pk2(o[2] * inv, o[3] * inv)};
  u32x2 w1 = {pk2(o[4] * inv, o[5] * inv), pk2(o[6] * inv, o[7] * inv)};
  *(u32x2*)(op) = w0;
  *(u32x2*)(op + 8) = w1;
}

__global__ __launch_bounds__(256) void k3_out(const u16* __restrict__ O, const u16* __restrict__ wo,
                                              const u16* __restrict__ x, const u16* __restrict__ xn,
                                              const float* __restrict__ ypart, const int* __restrict__ idx_in,
                                              const float* __restrict__ rsel_in, u16* __restrict__ out) {
  __shared__ __attribute__((aligned(16))) u16 wl[512 * 32];
  const int tid = threadIdx.x, wave = tid >> 6, lane = tid & 63;
  const int r = lane & 31, h = lane >> 5;
  const int b = blockIdx.x;
  const int nt = (b & 7) | (((b >> 3) & 1) << 3);
  const int mt = b >> 4;
#pragma unroll
  for (int i = 0; i < 8; ++i) {
    const int piece = (i * 4 + wave) * 64 + lane;
    const int k = piece >> 2, c = (piece & 3) * 8;
    const u16* src = wo + (size_t)k * 512 + nt * 32 + c;
    __builtin_amdgcn_global_load_lds((const unsigned*)src, (unsigned*)(wl + (i * 4 + wave) * 512), 16, 0, 0);
  }
  const int row = mt * 128 + wave * 32 + r;
  const u16* orow = O + (size_t)row * 512 + 8 * h;
  u32x4 oa[32];
#pragma unroll
  for (int t = 0; t < 32; ++t) oa[t] = *(const u32x4*)(orow + 16 * t);
  asm volatile("s_waitcnt vmcnt(0)" ::: "memory");
  __syncthreads();
  const int g = lane >> 4, i16 = lane & 15;
  const u16* lb = wl + (8 * (g >> 1) + (i16 >> 2)) * 32 + 16 * (g & 1) + 4 * (i16 & 3);
  f32x16 acc;
#pragma unroll
  for (int i = 0; i < 16; ++i) acc[i] = 0.f;
#pragma unroll
  for (int t = 0; t < 32; ++t) {
    bf16x8 wf = tr_frag(lb, 16 * t * 32, 32);
    acc = MFMA32(wf, __builtin_bit_cast(bf16x8, oa[t]), acc);
  }
  int sel = -1;
#pragma unroll
  for (int j = 0; j < 8; ++j)
    if (idx_in[j] == row) sel = j;
#pragma unroll
  for (int gq = 0; gq < 4; ++gq) {
    const int n = nt * 32 + 8 * gq + 4 * h;
    const size_t off = (size_t)row * 512 + n;
    const u32x2 xv = *(const u32x2*)(x + off);
    const u32x2 nv = *(const u32x2*)(xn + off);
    float xf[4] = {lo16(xv[0]), hi16(xv[0]), lo16(xv[1]), hi16(xv[1])};
    float nf[4] = {lo16(nv[0]), hi16(nv[0]), lo16(nv[1]), hi16(nv[1])};
    float res[4];
#pragma unroll
    for (int j = 0; j < 4; ++j) {
      float upd = 0.f;
      if (sel >= 0) {
        float y = 0.f;
        for (int pb = 0; pb < NMOE; ++pb) y += ypart[((size_t)pb * 8 + sel) * 512 + n + j];
        upd = rbf(rbf(y) * rsel_in[sel]);
      }
      const float a = rbf(acc[4 * gq + j]);
      const float bb = rbf(nf[j] + upd);
      res[j] = rbf(xf[j] + a) + bb;
    }
    u32x2 w = {pk2(res[0], res[1]), pk2(res[2], res[3])};
    *(u32x2*)(out + off) = w;
  }
}

extern "C" void kernel_launch(void* const* d_in, const int* in_sizes, int n_in, void* d_out, int out_size,
                              void* d_ws, size_t ws_size, hipStream_t stream) {
  const u16* x = (const u16*)d_in[0];
  const u16* wqkv = (const u16*)d_in[1];
  const u16* wo = (const u16*)d_in[2];
  const u16* w_r = (const u16*)d_in[4];
  const u16* wg = (const u16*)d_in[5];
  const u16* wu = (const u16*)d_in[6];
  const u16* wd = (const u16*)d_in[7];
  char* ws = (char*)d_ws;
  const size_t MB2 = (size_t)2048 * 512 * 2;
  u16* xn = (u16*)(ws);
  u16* Q = (u16*)(ws + MB2);
  u16* K = (u16*)(ws + 2 * MB2);
  u16* Vt = (u16*)(ws + 3 * MB2);
  u16* O = (u16*)(ws + 4 * MB2);
  float* r = (float*)(ws + 5 * MB2);
  int* idx = (int*)(ws + 5 * MB2 + 8192);
  float* rsel = (float*)(ws + 5 * MB2 + 8192 + 64);
  float* ypart = (float*)(ws + 5 * MB2 + 16384);
  k1_ln_qkv<<<256, 256, 0, stream>>>(x, wqkv, w_r, xn, r, Q, K, Vt);
  k2_attn<<<NMOE + 256, 512, 0, stream>>>(Q, K, Vt, O, r, xn, wg, wu, wd, ypart, idx, rsel);
  k3_out<<<256, 256, 0, stream>>>(O, wo, x, xn, ypart, idx, rsel, (u16*)d_out);
}
```

```cpp
#include <hip/hip_runtime.h>
#include <hip/hip_bf16.h>
#include <stdint.h>

typedef unsigned short u16;
typedef __attribute__((ext_vector_type(8))) short bf16x8;
typedef __attribute__((ext_vector_type(4))) short s16x4;
typedef __attribute__((ext_vector_type(16))) float f32x16;
typedef __attribute__((ext_vector_type(4))) unsigned u32x4;
typedef __attribute__((ext_vector_type(2))) unsigned u32x2;
typedef __attribute__((address_space(3))) s16x4 lds_s16x4;

#define DI __device__ __forceinline__
#define MFMA32(a, b, c) __builtin_amdgcn_mfma_f32_32x32x16_bf16((a), (b), (c), 0, 0, 0)

constexpr int S_ = 2048, D_ = 512, F_ = 1376, NH = 32;
constexpr int NMOE = 86;

#define STAMP(i)
DI float bf2f(u16 v) { return __uint_as_float(((unsigned)v) << 16); }
DI u16 f2bf(float f) { __bf16 b = (__bf16)f; return __builtin_bit_cast(u16, b); }
DI float rbf(float f) { return bf2f(f2bf(f)); }
DI float lo16(unsigned w) { return __uint_as_float(w << 16); }
DI float hi16(unsigned w) { return __uint_as_float(w & 0xffff0000u); }
DI unsigned pk2(float lo, float hi) { return (unsigned)f2bf(lo) | ((unsigned)f2bf(hi) << 16); }

DI bf16x8 tr_frag(const u16* lds_lane_base, int elem_off, int row_stride) {
  s16x4 a = __builtin_amdgcn_ds_read_tr16_b64_v4i16((lds_s16x4*)(lds_lane_base + elem_off));
  s16x4 b = __builtin_amdgcn_ds_read_tr16_b64_v4i16((lds_s16x4*)(lds_lane_base + elem_off + 4 * row_stride));
  return __builtin_shufflevector(a, b, 0, 1, 2, 3, 4, 5, 6, 7);
}

__global__ __launch_bounds__(256) void k0_ln(const u16* __restrict__ x, const u16* __restrict__ w_r,
                                             u16* __restrict__ xn_out, float* __restrict__ r_out,
                                             float* __restrict__ ypart) {
  const int lane = threadIdx.x & 63;
  if (blockIdx.x < 16) ypart[blockIdx.x * 256 + threadIdx.x] = 0.f;
  const int row = blockIdx.x * 4 + (threadIdx.x >> 6);
  const u32x4 xv = *(const u32x4*)(x + (size_t)row * 512 + lane * 8);
  const u32x4 wv = *(const u32x4*)(w_r + lane * 8);
  float f[8];
#pragma unroll
  for (int j = 0; j < 4; ++j) { f[2 * j] = lo16(xv[j]); f[2 * j + 1] = hi16(xv[j]); }
  float s = 0.f;
#pragma unroll
  for (int j = 0; j < 8; ++j) s += f[j];
#pragma unroll
  for (int o = 32; o > 0; o >>= 1) s += __shfl_xor(s, o);
  const float mean = s * (1.0f / 512.0f);
  float vs = 0.f;
#pragma unroll
  for (int j = 0; j < 8; ++j) { float d = f[j] - mean; vs += d * d; }
#pragma unroll
  for (int o = 32; o > 0; o >>= 1) vs += __shfl_xor(vs, o);
  const float m_b = rbf(mean), v_b = rbf(vs * (1.0f / 512.0f));
  const float den = rbf(sqrtf(rbf(v_b + 1e-5f)));
  u32x4 ov;
  float rs = 0.f;
#pragma unroll
  for (int j = 0; j < 4; ++j) {
    float a0 = rbf(rbf(f[2 * j] - m_b) / den), a1 = rbf(rbf(f[2 * j + 1] - m_b) / den);
    ov[j] = pk2(a0, a1);
    rs += a0 * lo16(wv[j]) + a1 * hi16(wv[j]);
  }
  *(u32x4*)(xn_out + (size_t)row * 512 + lane * 8) = ov;
#pragma unroll
  for (int o = 32; o > 0; o >>= 1) rs += __shfl_xor(rs, o);
  if (lane == 0) r_out[row] = rbf(rs);
}

__global__ __launch_bounds__(256) void k1_qkv(const u16* __restrict__ xn, const u16* __restrict__ wqkv,
                                              u16* __restrict__ Qo, u16* __restrict__ Ko, u16* __restrict__ Vt) {
  __shared__ __attribute__((aligned(16))) u16 wl[512 * 96];
  const int tid = threadIdx.x, wave = tid >> 6, lane = tid & 63;
  const int r = lane & 31, h = lane >> 5;
  const int b = blockIdx.x;
  const int nt = (b & 7) | (((b >> 3) & 1) << 3);
  const int mt = b >> 4;

#pragma unroll
  for (int i = 0; i < 24; ++i) {
    const int piece = (i * 4 + wave) * 64 + lane;
    const int k = piece / 12, c = (piece % 12) * 8;
    const u16* src = wqkv + (size_t)k * 1536 + nt * 96 + c;
    __builtin_amdgcn_global_load_lds((const unsigned*)src, (unsigned*)(wl + (i * 4 + wave) * 512), 16, 0, 0);
  }
  STAMP(0);
  const int row = mt * 128 + wave * 32 + r;
  const u16* xr = xn + (size_t)row * 512 + 8 * h;
  u32x4 xa[32];
#pragma unroll
  for (int t = 0; t < 32; ++t) xa[t] = *(const u32x4*)(xr + 16 * t);
  STAMP(1);
  STAMP(2);
  STAMP(3);
  asm volatile("s_waitcnt vmcnt(0)" ::: "memory");
  __syncthreads();
  STAMP(4);

  const int g = lane >> 4, i16 = lane & 15;
  const u16* lb = wl + (8 * (g >> 1) + (i16 >> 2)) * 96 + 16 * (g & 1) + 4 * (i16 & 3);
  f32x16 acc[3];
#pragma unroll
  for (int c = 0; c < 3; ++c)
#pragma unroll
    for (int i = 0; i < 16; ++i) acc[c][i] = 0.f;
  bool isv[3];
#pragma unroll
  for (int c = 0; c < 3; ++c) isv[c] = (nt * 96 + c * 32) >= 1024;

#pragma unroll
  for (int t = 0; t < 32; ++t) {
    const bf16x8 xf = __builtin_bit_cast(bf16x8, xa[t]);
#pragma unroll
    for (int c = 0; c < 3; ++c) {
      bf16x8 wf = tr_frag(lb, 16 * t * 96 + c * 32, 96);
      const bf16x8 aop = isv[c] ? xf : wf;
      const bf16x8 bop = isv[c] ? wf : xf;
      acc[c] = MFMA32(aop, bop, acc[c]);
    }
  }

  STAMP(5);
  const int tok0 = mt * 128 + wave * 32;
#pragma unroll
  for (int c = 0; c < 3; ++c) {
    const int col0 = nt * 96 + c * 32;
    const int sec = col0 >> 9, hp = (col0 & 511) >> 4;
    if (sec < 2) {
      u16* dst = sec == 0 ? Qo : Ko;
#pragma unroll
      for (int gq = 0; gq < 4; ++gq) {
        const int head = hp + (gq >> 1), d = 8 * (gq & 1) + 4 * h;
        u32x2 v = {pk2(acc[c][4 * gq], acc[c][4 * gq + 1]), pk2(acc[c][4 * gq + 2], acc[c][4 * gq + 3])};
        *(u32x2*)(dst + ((size_t)(head * 2048 + tok0 + r) * 16 + d)) = v;
      }
    } else {
      const int head = hp + (r >> 4), d = r & 15;
#pragma unroll
      for (int gq = 0; gq < 4; ++gq) {
        u32x2 v = {pk2(acc[c][4 * gq], acc[c][4 * gq + 1]), pk2(acc[c][4 * gq + 2], acc[c][4 * gq + 3])};
        *(u32x2*)(Vt + ((size_t)(head * 16 + d) * 2048 + tok0 + 8 * gq + 4 * h)) = v;
      }
    }
  }
  STAMP(6);
}

DI void moe_block(int mb, const float* __restrict__ r_in, const u16* __restrict__ xn, const u16* __restrict__ wg,
                  const u16* __restrict__ wu, const u16* __restrict__ wd, float* __restrict__ ypart,
                  int* __restrict__ idx_out, float* __restrict__ rsel_out, u16* wls) {
  float* xs = (float*)(wls + 3 * 8192);
  float* gu = xs + 8 * 512;
  float* hs = gu + 2 * 8 * 16;
  float* redv = hs + 8 * 16;
  int* redi = (int*)(redv + 8);
  int* sidx = redi + 8;
  const int tid = threadIdx.x, wave = tid >> 6, lane = tid & 63;
  const int f0 = mb * 16;
#pragma unroll
  for (int i = 0; i < 2; ++i) {
    const int piece = i * 512 + tid;
    const int k = piece >> 1, c = (piece & 1) * 8;
    __builtin_amdgcn_global_load_lds((const unsigned*)(wg + (size_t)k * F_ + f0 + c),
                                     (unsigned*)(wls + (i * 8 + wave) * 512), 16, 0, 0);
    __builtin_amdgcn_global_load_lds((const unsigned*)(wu + (size_t)k * F_ + f0 + c),
                                     (unsigned*)(wls + 8192 + (i * 8 + wave) * 512), 16, 0, 0);
    __builtin_amdgcn_global_load_lds((const unsigned*)(wd + (size_t)f0 * 512 + (size_t)piece * 8),
                                     (unsigned*)(wls + 16384 + (i * 8 + wave) * 512), 16, 0, 0);
  }
  float v[4];
#pragma unroll
  for (int j = 0; j < 4; ++j) v[j] = r_in[tid * 4 + j];
  for (int it = 0; it < 8; ++it) {
    float bv = v[0];
    int bi = tid * 4;
#pragma unroll
    for (int j = 1; j < 4; ++j)
      if (v[j] > bv) { bv = v[j]; bi = tid * 4 + j; }
#pragma unroll
    for (int o = 32; o > 0; o >>= 1) {
      float ov = __shfl_xor(bv, o);
      int oi = __shfl_xor(bi, o);
      if (ov > bv || (ov == bv && oi < bi)) { bv = ov; bi = oi; }
    }
    if (lane == 0) { redv[wave] = bv; redi[wave] = bi; }
    __syncthreads();
    float fv = redv[0];
    int fi = redi[0];
#pragma unroll
    for (int w = 1; w < 8; ++w)
      if (redv[w] > fv || (redv[w] == fv && redi[w] < fi)) { fv = redv[w]; fi = redi[w]; }
    if (tid == 0) {
      sidx[it] = fi;
      if (mb == 0) { idx_out[it] = fi; rsel_out[it] = rbf(1.0f / (1.0f + __expf(-fv))); }
    }
#pragma unroll
    for (int j = 0; j < 4; ++j)
      if (tid * 4 + j == fi) v[j] = -INFINITY;
    __syncthreads();
  }
#pragma unroll
  for (int tk = 0; tk < 8; ++tk) xs[tk * 512 + tid] = bf2f(xn[(size_t)sidx[tk] * 512 + tid]);
  asm volatile("s_waitcnt vmcnt(0)" ::: "memory");
  __syncthreads();
  {
    const int khf = tid >> 8, mat = (tid >> 7) & 1, tok = (tid >> 4) & 7, col = tid & 15;
    const u16* w = wls + mat * 8192 + khf * 256 * 16 + col;
    const float* xr = xs + tok * 512 + khf * 256;
    float acc = 0.f;
#pragma unroll 16
    for (int k = 0; k < 256; ++k) acc += xr[k] * bf2f(w[k * 16]);
    if (khf == 0) gu[(mat * 8 + tok) * 16 + col] = acc;
    __syncthreads();
    if (khf == 1) gu[(mat * 8 + tok) * 16 + col] += acc;
  }
  __syncthreads();
  if (tid < 128) {
    const int tok = tid >> 4, col = tid & 15;
    float gg = rbf(gu[tok * 16 + col]);
    float g = rbf(gg / (1.0f + __expf(-gg)));
    float u = rbf(gu[(8 + tok) * 16 + col]);
    hs[tok * 16 + col] = rbf(g * u);
  }
  __syncthreads();
  float acc[8];
#pragma unroll
  for (int tk = 0; tk < 8; ++tk) acc[tk] = 0.f;
#pragma unroll 4
  for (int f = 0; f < 16; ++f) {
    float w = bf2f(wls[16384 + f * 512 + tid]);
#pragma unroll
    for (int tk = 0; tk < 8; ++tk) acc[tk] += hs[tk * 16 + f] * w;
  }
#pragma unroll
  for (int tk = 0; tk < 8; ++tk) atomicAdd(&ypart[tk * 512 + tid], acc[tk]);
}

DI unsigned cvtpk(float lo, float hi) {
  unsigned r;
  asm volatile("v_cvt_pk_bf16_f32 %0, %1, %2" : "=v"(r) : "v"(lo), "v"(hi));
  return r;
}
DI bf16x8 pack8(const f32x16& x, int s) {
  u32x4 p;
  asm volatile("v_cvt_pk_bf16_f32 %0, %4, %5\n\tv_cvt_pk_bf16_f32 %1, %6, %7\n\tv_cvt_pk_bf16_f32 %2, %8, %9\n\tv_cvt_pk_bf16_f32 %3, %10, %11\n\ts_nop 1"
               : "=&v"(p[0]), "=&v"(p[1]), "=&v"(p[2]), "=&v"(p[3])
               : "v"(x[8 * s]), "v"(x[8 * s + 1]), "v"(x[8 * s + 2]), "v"(x[8 * s + 3]), "v"(x[8 * s + 4]),
                 "v"(x[8 * s + 5]), "v"(x[8 * s + 6]), "v"(x[8 * s + 7]));
  return __builtin_bit_cast(bf16x8, p);
}
DI float max3(float a, float b, float c) {
  float r;
  asm volatile("v_max3_f32 %0, %1, %2, %3" : "=v"(r) : "v"(a), "v"(b), "v"(c));
  return r;
}

__global__ __launch_bounds__(512) void k2_attn(const u16* __restrict__ Q, const u16* __restrict__ K,
                                               const u16* __restrict__ Vt, u16* __restrict__ O,
                                               const float* __restrict__ r_in, const u16* __restrict__ xn,
                                               const u16* __restrict__ wg, const u16* __restrict__ wu,
                                               const u16* __restrict__ wd, float* __restrict__ ypart,
                                               int* __restrict__ idx_out, float* __restrict__ rsel_out) {
  __shared__ __attribute__((aligned(16))) u16 smem[3 * 8192 + 2 * (8 * 512 + 2 * 8 * 16 + 8 * 16 + 64)];
  int b = blockIdx.x;
  if (b < NMOE) {
    moe_block(b, r_in, xn, wg, wu, wd, ypart, idx_out, rsel_out, smem);
    return;
  }
  b -= NMOE;
  const int tid = threadIdx.x, wave = tid >> 6, lane = tid & 63;
  const int r = lane & 31, h = lane >> 5;
  const int head = (b & 7) * 4 + ((b >> 3) & 3);
  const int qb = b >> 5;
  const int qt = wave & 3, kh = wave >> 2;
  const int q0 = qb * 128 + qt * 32;

  const bf16x8 qf = *(const bf16x8*)(Q + ((size_t)(head * 2048 + q0 + r) * 16 + 8 * h));
  const int pir = (r & 0x13) | ((r & 4) << 1) | ((r & 8) >> 1);
  const u16* kp = K + ((size_t)(head * 2048 + kh * 1024 + pir) * 16 + 8 * h);
  const u16* vp = Vt + ((size_t)(head * 16 + (r & 15)) * 2048 + kh * 1024 + 8 * h);
  const short one = (short)0x3f80;
  const short vc = (r == 16) ? one : (short)0;
  const bf16x8 vconst = {vc, vc, vc, vc, vc, vc, vc, vc};

  constexpr float C = 0.25f * 1.4426950408889634f;
  float m = -INFINITY;
  f32x16 o;
#pragma unroll
  for (int i = 0; i < 16; ++i) o[i] = 0.f;
  f32x16 zero;
#pragma unroll
  for (int i = 0; i < 16; ++i) zero[i] = 0.f;

  bf16x8 k0, k1, v0 = vconst, v1 = vconst, v2 = vconst, v3 = vconst;
  k0 = *(const bf16x8*)(kp);
  k1 = *(const bf16x8*)(kp + 512);
  if (r < 16) {
    v0 = *(const bf16x8*)(vp);
    v1 = *(const bf16x8*)(vp + 16);
    v2 = *(const bf16x8*)(vp + 32);
    v3 = *(const bf16x8*)(vp + 48);
  }
  for (int kt = 0; kt < 16; ++kt) {
    const int kn = kt < 15 ? kt + 1 : 15;
    f32x16 s0 = MFMA32(k0, qf, zero);
    f32x16 s1 = MFMA32(k1, qf, zero);
    const bf16x8 c0 = v0, c1 = v1, c2 = v2, c3 = v3;
    k0 = *(const bf16x8*)(kp + (size_t)kn * 1024);
    k1 = *(const bf16x8*)(kp + (size_t)kn * 1024 + 512);
    if (r < 16) {
      v0 = *(const bf16x8*)(vp + kn * 64);
      v1 = *(const bf16x8*)(vp + kn * 64 + 16);
      v2 = *(const bf16x8*)(vp + kn * 64 + 32);
      v3 = *(const bf16x8*)(vp + kn * 64 + 48);
    }
    float t0, t1, t2, t3;
    __builtin_amdgcn_sched_barrier(0);
    asm volatile("s_nop 7\n\ts_nop 7" ::: "memory");
    __builtin_amdgcn_sched_barrier(0);
    t0 = max3(s0[0], s0[1], s0[2]);
    t1 = max3(s0[3], s0[4], s0[5]);
    t2 = max3(s1[0], s1[1], s1[2]);
    t3 = max3(s1[3], s1[4], s1[5]);
    t0 = max3(t0, s0[6], s0[7]);
    t1 = max3(t1, s0[8], s0[9]);
    t2 = max3(t2, s1[6], s1[7]);
    t3 = max3(t3, s1[8], s1[9]);
    t0 = max3(t0, s0[10], s0[11]);
    t1 = max3(t1, s0[12], s0[13]);
    t2 = max3(t2, s1[10], s1[11]);
    t3 = max3(t3, s1[12], s1[13]);
    t0 = max3(t0, s0[14], s0[15]);
    t2 = max3(t2, s1[14], s1[15]);
    t0 = max3(t0, t1, t2);
    float tmax = max3(t0, t3, t3);
    {
      auto rr = __builtin_amdgcn_permlane32_swap(__float_as_uint(tmax), __float_as_uint(tmax), false, false);
      tmax = max3(__uint_as_float(rr[0]), __uint_as_float(rr[1]), tmax);
    }
    if (__any(tmax > m)) {
      const float mn = fmaxf(m, tmax);
      const float alpha = __builtin_amdgcn_exp2f((m - mn) * C);
#pragma unroll
      for (int i = 0; i < 9; ++i) o[i] *= alpha;
      m = mn;
    }
    const float mc = -m * C;
#pragma unroll
    for (int i = 0; i < 16; ++i) {
      s0[i] = __builtin_amdgcn_exp2f(fmaf(s0[i], C, mc));
      s1[i] = __builtin_amdgcn_exp2f(fmaf(s1[i], C, mc));
    }
    o = MFMA32(c0, pack8(s0, 0), o);
    o = MFMA32(c1, pack8(s0, 1), o);
    o = MFMA32(c2, pack8(s1, 0), o);
    o = MFMA32(c3, pack8(s1, 1), o);
  }
  float* cb = (float*)smem;
  if (kh == 1) {
#pragma unroll
    for (int i = 0; i < 9; ++i) cb[(qt * 10 + i) * 64 + lane] = o[i];
    cb[(qt * 10 + 9) * 64 + lane] = m;
  }
  __syncthreads();
  if (kh == 0) {
    const float m2 = cb[(qt * 10 + 9) * 64 + lane];
    const float mn = fmaxf(m, m2);
    const float a1 = __builtin_amdgcn_exp2f((m - mn) * C), a2 = __builtin_amdgcn_exp2f((m2 - mn) * C);
    float of[9];
#pragma unroll
    for (int i = 0; i < 9; ++i) of[i] = o[i] * a1 + cb[(qt * 10 + i) * 64 + lane] * a2;
    const float l = __shfl(of[8], r);
    const float inv = 1.0f / l;
    u16* op = O + (size_t)(q0 + r) * 512 + head * 16 + 4 * h;
    u32x2 w0 = {pk2(of[0] * inv, of[1] * inv), pk2(of[2] * inv, of[3] * inv)};
    u32x2 w1 = {pk2(of[4] * inv, of[5] * inv), pk2(of[6] * inv, of[7] * inv)};
    *(u32x2*)(op) = w0;
    *(u32x2*)(op + 8) = w1;
  }
}

__global__ __launch_bounds__(256) void k3_out(const u16* __restrict__ O, const u16* __restrict__ wo,
                                              const u16* __restrict__ x, const u16* __restrict__ xn,
                                              const float* __restrict__ ypart, const int* __restrict__ idx_in,
                                              const float* __restrict__ rsel_in, u16* __restrict__ out) {
  __shared__ __attribute__((aligned(16))) u16 wl[512 * 32];
  const int tid = threadIdx.x, wave = tid >> 6, lane = tid & 63;
  const int r = lane & 31, h = lane >> 5;
  const int b = blockIdx.x;
  const int nt = (b & 7) | (((b >> 3) & 1) << 3);
  const int mt = b >> 4;
#pragma unroll
  for (int i = 0; i < 8; ++i) {
    const int piece = (i * 4 + wave) * 64 + lane;
    const int k = piece >> 2, c = (piece & 3) * 8;
    const u16* src = wo + (size_t)k * 512 + nt * 32 + c;
    __builtin_amdgcn_global_load_lds((const unsigned*)src, (unsigned*)(wl + (i * 4 + wave) * 512), 16, 0, 0);
  }
  const int row = mt * 128 + wave * 32 + r;
  const u16* orow = O + (size_t)row * 512 + 8 * h;
  u32x4 oa[32];
#pragma unroll
  for (int t = 0; t < 32; ++t) oa[t] = *(const u32x4*)(orow + 16 * t);
  asm volatile("s_waitcnt vmcnt(0)" ::: "memory");
  __syncthreads();
  const int g = lane >> 4, i16 = lane & 15;
  const u16* lb = wl + (8 * (g >> 1) + (i16 >> 2)) * 32 + 16 * (g & 1) + 4 * (i16 & 3);
  f32x16 acc;
#pragma unroll
  for (int i = 0; i < 16; ++i) acc[i] = 0.f;
#pragma unroll
  for (int t = 0; t < 32; ++t) {
    bf16x8 wf = tr_frag(lb, 16 * t * 32, 32);
    acc = MFMA32(wf, __builtin_bit_cast(bf16x8, oa[t]), acc);
  }
  int sel = -1;
#pragma unroll
  for (int j = 0; j < 8; ++j)
    if (idx_in[j] == row) sel = j;
  const float rs_sel = rsel_in[sel >= 0 ? sel : 0];
#pragma unroll
  for (int gq = 0; gq < 4; ++gq) {
    const int n = nt * 32 + 8 * gq + 4 * h;
    const size_t off = (size_t)row * 512 + n;
    const u32x2 xv = *(const u32x2*)(x + off);
    const u32x2 nv = *(const u32x2*)(xn + off);
    float xf[4] = {lo16(xv[0]), hi16(xv[0]), lo16(xv[1]), hi16(xv[1])};
    float nf[4] = {lo16(nv[0]), hi16(nv[0]), lo16(nv[1]), hi16(nv[1])};
    float res[4];
    const float4 ysum = *(const float4*)(ypart + (sel >= 0 ? sel : 0) * 512 + n);
    const float ys[4] = {ysum.x, ysum.y, ysum.z, ysum.w};
#pragma unroll
    for (int j = 0; j < 4; ++j) {
      const float upd = sel >= 0 ? rbf(rbf(ys[j]) * rs_sel) : 0.f;
      const float a = rbf(acc[4 * gq + j]);
      const float bb = rbf(nf[j] + upd);
      res[j] = rbf(xf[j] + a) + bb;
    }
    u32x2 w = {pk2(res[0], res[1]), pk2(res[2], res[3])};
    *(u32x2*)(out + off) = w;
  }
}

extern "C" void kernel_launch(void* const* d_in, const int* in_sizes, int n_in, void* d_out, int out_size,
                              void* d_ws, size_t ws_size, hipStream_t stream) {
  const u16* x = (const u16*)d_in[0];
  const u16* wqkv = (const u16*)d_in[1];
  const u16* wo = (const u16*)d_in[2];
  const u16* w_r = (const u16*)d_in[4];
  const u16* wg = (const u16*)d_in[5];
  const u16* wu = (const u16*)d_in[6];
  const u16* wd = (const u16*)d_in[7];
  char* ws = (char*)d_ws;
  const size_t MB2 = (size_t)2048 * 512 * 2;
  u16* xn = (u16*)(ws);
  u16* Q = (u16*)(ws + MB2);
  u16* K = (u16*)(ws + 2 * MB2);
  u16* Vt = (u16*)(ws + 3 * MB2);
  u16* O = (u16*)(ws + 4 * MB2);
  float* r = (float*)(ws + 5 * MB2);
  int* idx = (int*)(ws + 5 * MB2 + 8192);
  float* rsel = (float*)(ws + 5 * MB2 + 8192 + 64);
  float* ypart = (float*)(ws + 5 * MB2 + 16384);
  k0_ln<<<512, 256, 0, stream>>>(x, w_r, xn, r, ypart);
  k1_qkv<<<256, 256, 0, stream>>>(xn, wqkv, Q, K, Vt);
  k2_attn<<<NMOE + 512, 512, 0, stream>>>(Q, K, Vt, O, r, xn, wg, wu, wd, ypart, idx, rsel);
  k3_out<<<256, 256, 0, stream>>>(O, wo, x, xn, ypart, idx, rsel, (u16*)d_out);
}
```

```cpp
#include <hip/hip_runtime.h>
#include <hip/hip_bf16.h>
#include <stdint.h>

typedef unsigned short u16;
typedef __attribute__((ext_vector_type(8))) short bf16x8;
typedef __attribute__((ext_vector_type(4))) short s16x4;
typedef __attribute__((ext_vector_type(16))) float f32x16;
typedef __attribute__((ext_vector_type(4))) unsigned u32x4;
typedef __attribute__((ext_vector_type(2))) unsigned u32x2;
typedef __attribute__((address_space(3))) s16x4 lds_s16x4;

#define DI __device__ __forceinline__
#define MFMA32(a, b, c) __builtin_amdgcn_mfma_f32_32x32x16_bf16((a), (b), (c), 0, 0, 0)

constexpr int S_ = 2048, D_ = 512, F_ = 1376, NH = 32;
constexpr int NMOE = 86;

#define STAMP(i)
DI float bf2f(u16 v) { return __uint_as_float(((unsigned)v) << 16); }
DI u16 f2bf(float f) { __bf16 b = (__bf16)f; return __builtin_bit_cast(u16, b); }
DI float rbf(float f) { return bf2f(f2bf(f)); }
DI float lo16(unsigned w) { return __uint_as_float(w << 16); }
DI float hi16(unsigned w) { return __uint_as_float(w & 0xffff0000u); }
DI unsigned pk2(float lo, float hi) { return (unsigned)f2bf(lo) | ((unsigned)f2bf(hi) << 16); }

DI bf16x8 tr_frag(const u16* lds_lane_base, int elem_off, int row_stride) {
  s16x4 a = __builtin_amdgcn_ds_read_tr16_b64_v4i16((lds_s16x4*)(lds_lane_base + elem_off));
  s16x4 b = __builtin_amdgcn_ds_read_tr16_b64_v4i16((lds_s16x4*)(lds_lane_base + elem_off + 4 * row_stride));
  return __builtin_shufflevector(a, b, 0, 1, 2, 3, 4, 5, 6, 7);
}

__global__ __launch_bounds__(256) void k0_ln(const u16* __restrict__ x, const u16* __restrict__ w_r,
                                             u16* __restrict__ xn_out, float* __restrict__ r_out,
                                             float* __restrict__ ypart) {
  const int lane = threadIdx.x & 63;
  if (blockIdx.x < 16) ypart[blockIdx.x * 256 + threadIdx.x] = 0.f;
  const int row = blockIdx.x * 4 + (threadIdx.x >> 6);
  const u32x4 xv = *(const u32x4*)(x + (size_t)row * 512 + lane * 8);
  const u32x4 wv = *(const u32x4*)(w_r + lane * 8);
  float f[8];
#pragma unroll
  for (int j = 0; j < 4; ++j) { f[2 * j] = lo16(xv[j]); f[2 * j + 1] = hi16(xv[j]); }
  float s = 0.f;
#pragma unroll
  for (int j = 0; j < 8; ++j) s += f[j];
#pragma unroll
  for (int o = 32; o > 0; o >>= 1) s += __shfl_xor(s, o);
  const float mean = s * (1.0f / 512.0f);
  float vs = 0.f;
#pragma unroll
  for (int j = 0; j < 8; ++j) { float d = f[j] - mean; vs += d * d; }
#pragma unroll
  for (int o = 32; o > 0; o >>= 1) vs += __shfl_xor(vs, o);
  const float m_b = rbf(mean), v_b = rbf(vs * (1.0f / 512.0f));
  const float den = rbf(sqrtf(rbf(v_b + 1e-5f)));
  u32x4 ov;
  float rs = 0.f;
#pragma unroll
  for (int j = 0; j < 4; ++j) {
    float a0 = rbf(rbf(f[2 * j] - m_b) / den), a1 = rbf(rbf(f[2 * j + 1] - m_b) / den);
    ov[j] = pk2(a0, a1);
    rs += a0 * lo16(wv[j]) + a1 * hi16(wv[j]);
  }
  *(u32x4*)(xn_out + (size_t)row * 512 + lane * 8) = ov;
#pragma unroll
  for (int o = 32; o > 0; o >>= 1) rs += __shfl_xor(rs, o);
  if (lane == 0) r_out[row] = rbf(rs);
}

DI void top8_block(const float* __restrict__ r_in, int* __restrict__ idx_out, float* __restrict__ rsel_out,
                   float* scratch) {
  float* tmx = scratch;
  float* thr = scratch + 256;
  int* cnt = (int*)(scratch + 257);
  float* cv = scratch + 260;
  int* ci = (int*)(scratch + 260 + 2048);
  const int tid = threadIdx.x;
  float v[8];
#pragma unroll
  for (int j = 0; j < 8; ++j) v[j] = r_in[tid * 8 + j];
  float mx = v[0];
#pragma unroll
  for (int j = 1; j < 8; ++j) mx = fmaxf(mx, v[j]);
  tmx[tid] = mx;
  if (tid == 0) *cnt = 0;
  __syncthreads();
  int rank = 0;
  for (int j = 0; j < 256; ++j) {
    const float o = tmx[j];
    rank += (o > mx || (o == mx && j < tid)) ? 1 : 0;
  }
  if (rank == 7) *thr = mx;
  __syncthreads();
  const float T = *thr;
#pragma unroll
  for (int j = 0; j < 8; ++j)
    if (v[j] >= T) {
      const int pos = atomicAdd(cnt, 1);
      cv[pos] = v[j];
      ci[pos] = tid * 8 + j;
    }
  __syncthreads();
  const int n = *cnt;
  for (int c = tid; c < n; c += 256) {
    const float mv = cv[c];
    const int mi = ci[c];
    int rk = 0;
    for (int j = 0; j < n; ++j) rk += (cv[j] > mv || (cv[j] == mv && ci[j] < mi)) ? 1 : 0;
    if (rk < 8) { idx_out[rk] = mi; rsel_out[rk] = rbf(1.0f / (1.0f + __expf(-mv))); }
  }
}

__global__ __launch_bounds__(256) void k1_qkv(const u16* __restrict__ xn, const u16* __restrict__ wqkv,
                                              u16* __restrict__ Qo, u16* __restrict__ Ko, u16* __restrict__ Vt,
                                              const float* __restrict__ r_in, int* __restrict__ idx_out,
                                              float* __restrict__ rsel_out) {
  __shared__ __attribute__((aligned(16))) u16 wl[512 * 96];
  if (blockIdx.x == 256) {
    top8_block(r_in, idx_out, rsel_out, (float*)wl);
    return;
  }
  const int tid = threadIdx.x, wave = tid >> 6, lane = tid & 63;
  const int r = lane & 31, h = lane >> 5;
  const int b = blockIdx.x;
  const int nt = (b & 7) | (((b >> 3) & 1) << 3);
  const int mt = b >> 4;

#pragma unroll
  for (int i = 0; i < 24; ++i) {
    const int piece = (i * 4 + wave) * 64 + lane;
    const int k = piece / 12, c = (piece % 12) * 8;
    const u16* src = wqkv + (size_t)k * 1536 + nt * 96 + c;
    __builtin_amdgcn_global_load_lds((const unsigned*)src, (unsigned*)(wl + (i * 4 + wave) * 512), 16, 0, 0);
  }
  STAMP(0);
  const int row = mt * 128 + wave * 32 + r;
  const u16* xr = xn + (size_t)row * 512 + 8 * h;
  u32x4 xa[32];
#pragma unroll
  for (int t = 0; t < 32; ++t) xa[t] = *(const u32x4*)(xr + 16 * t);
  STAMP(1);
  STAMP(2);
  STAMP(3);
  asm volatile("s_waitcnt vmcnt(0)" ::: "memory");
  __syncthreads();
  STAMP(4);

  const int g = lane >> 4, i16 = lane & 15;
  const u16* lb = wl + (8 * (g >> 1) + (i16 >> 2)) * 96 + 16 * (g & 1) + 4 * (i16 & 3);
  f32x16 acc[3];
#pragma unroll
  for (int c = 0; c < 3; ++c)
#pragma unroll
    for (int i = 0; i < 16; ++i) acc[c][i] = 0.f;
  bool isv[3];
#pragma unroll
  for (int c = 0; c < 3; ++c) isv[c] = (nt * 96 + c * 32) >= 1024;

#pragma unroll
  for (int t = 0; t < 32; ++t) {
    const bf16x8 xf = __builtin_bit_cast(bf16x8, xa[t]);
#pragma unroll
    for (int c = 0; c < 3; ++c) {
      bf16x8 wf = tr_frag(lb, 16 * t * 96 + c * 32, 96);
      const bf16x8 aop = isv[c] ? xf : wf;
      const bf16x8 bop = isv[c] ? wf : xf;
      acc[c] = MFMA32(aop, bop, acc[c]);
    }
  }

  STAMP(5);
  const int tok0 = mt * 128 + wave * 32;
#pragma unroll
  for (int c = 0; c < 3; ++c) {
    const int col0 = nt * 96 + c * 32;
    const int sec = col0 >> 9, hp = (col0 & 511) >> 4;
    if (sec < 2) {
      u16* dst = sec == 0 ? Qo : Ko;
#pragma unroll
      for (int gq = 0; gq < 4; ++gq) {
        const int head = hp + (gq >> 1), d = 8 * (gq & 1) + 4 * h;
        u32x2 v = {pk2(acc[c][4 * gq], acc[c][4 * gq + 1]), pk2(acc[c][4 * gq + 2], acc[c][4 * gq + 3])};
        *(u32x2*)(dst + ((size_t)(head * 2048 + tok0 + r) * 16 + d)) = v;
      }
    } else {
      const int head = hp + (r >> 4), d = r & 15;
#pragma unroll
      for (int gq = 0; gq < 4; ++gq) {
        u32x2 v = {pk2(acc[c][4 * gq], acc[c][4 * gq + 1]), pk2(acc[c][4 * gq + 2], acc[c][4 * gq + 3])};
        *(u32x2*)(Vt + ((size_t)(head * 16 + d) * 2048 + tok0 + 8 * gq + 4 * h)) = v;
      }
    }
  }
  STAMP(6);
}

DI void moe_block(int mb, const float* __restrict__ r_in, const u16* __restrict__ xn, const u16* __restrict__ wg,
                  const u16* __restrict__ wu, const u16* __restrict__ wd, float* __restrict__ ypart,
                  int* __restrict__ idx_out, float* __restrict__ rsel_out, u16* wls) {
  float* xs = (float*)(wls + 3 * 8192);
  float* gu = xs + 8 * 512;
  float* hs = gu + 2 * 8 * 16;
  float* redv = hs + 8 * 16;
  int* redi = (int*)(redv + 8);
  int* sidx = redi + 8;
  const int tid = threadIdx.x, wave = tid >> 6, lane = tid & 63;
  const int f0 = mb * 16;
#pragma unroll
  for (int i = 0; i < 2; ++i) {
    const int piece = i * 512 + tid;
    const int k = piece >> 1, c = (piece & 1) * 8;
    __builtin_amdgcn_global_load_lds((const unsigned*)(wg + (size_t)k * F_ + f0 + c),
                                     (unsigned*)(wls + (i * 8 + wave) * 512), 16, 0, 0);
    __builtin_amdgcn_global_load_lds((const unsigned*)(wu + (size_t)k * F_ + f0 + c),
                                     (unsigned*)(wls + 8192 + (i * 8 + wave) * 512), 16, 0, 0);
    __builtin_amdgcn_global_load_lds((const unsigned*)(wd + (size_t)f0 * 512 + (size_t)piece * 8),
                                     (unsigned*)(wls + 16384 + (i * 8 + wave) * 512), 16, 0, 0);
  }
#pragma unroll
  for (int tk = 0; tk < 8; ++tk) xs[tk * 512 + tid] = bf2f(xn[(size_t)idx_out[tk] * 512 + tid]);
  asm volatile("s_waitcnt vmcnt(0)" ::: "memory");
  __syncthreads();
  {
    const int khf = tid >> 8, mat = (tid >> 7) & 1, tok = (tid >> 4) & 7, col = tid & 15;
    const u16* w = wls + mat * 8192 + khf * 256 * 16 + col;
    const float* xr = xs + tok * 512 + khf * 256;
    float acc = 0.f;
#pragma unroll 16
    for (int k = 0; k < 256; ++k) acc += xr[k] * bf2f(w[k * 16]);
    if (khf == 0) gu[(mat * 8 + tok) * 16 + col] = acc;
    __syncthreads();
    if (khf == 1) gu[(mat * 8 + tok) * 16 + col] += acc;
  }
  __syncthreads();
  if (tid < 128) {
    const int tok = tid >> 4, col = tid & 15;
    float gg = rbf(gu[tok * 16 + col]);
    float g = rbf(gg / (1.0f + __expf(-gg)));
    float u = rbf(gu[(8 + tok) * 16 + col]);
    hs[tok * 16 + col] = rbf(g * u);
  }
  __syncthreads();
  float acc[8];
#pragma unroll
  for (int tk = 0; tk < 8; ++tk) acc[tk] = 0.f;
#pragma unroll 4
  for (int f = 0; f < 16; ++f) {
    float w = bf2f(wls[16384 + f * 512 + tid]);
#pragma unroll
    for (int tk = 0; tk < 8; ++tk) acc[tk] += hs[tk * 16 + f] * w;
  }
#pragma unroll
  for (int tk = 0; tk < 8; ++tk) atomicAdd(&ypart[tk * 512 + tid], acc[tk]);
}

DI unsigned cvtpk(float lo, float hi) {
  unsigned r;
  asm volatile("v_cvt_pk_bf16_f32 %0, %1, %2" : "=v"(r) : "v"(lo), "v"(hi));
  return r;
}
DI bf16x8 pack8(const f32x16& x, int s) {
  u32x4 p;
  asm volatile("v_cvt_pk_bf16_f32 %0, %4, %5\n\tv_cvt_pk_bf16_f32 %1, %6, %7\n\tv_cvt_pk_bf16_f32 %2, %8, %9\n\tv_cvt_pk_bf16_f32 %3, %10, %11\n\ts_nop 1"
               : "=&v"(p[0]), "=&v"(p[1]), "=&v"(p[2]), "=&v"(p[3])
               : "v"(x[8 * s]), "v"(x[8 * s + 1]), "v"(x[8 * s + 2]), "v"(x[8 * s + 3]), "v"(x[8 * s + 4]),
                 "v"(x[8 * s + 5]), "v"(x[8 * s + 6]), "v"(x[8 * s + 7]));
  return __builtin_bit_cast(bf16x8, p);
}
DI float max3(float a, float b, float c) {
  float r;
  asm volatile("v_max3_f32 %0, %1, %2, %3" : "=v"(r) : "v"(a), "v"(b), "v"(c));
  return r;
}

__global__ __launch_bounds__(512) void k2_attn(const u16* __restrict__ Q, const u16* __restrict__ K,
                                               const u16* __restrict__ Vt, u16* __restrict__ O,
                                               const float* __restrict__ r_in, const u16* __restrict__ xn,
                                               const u16* __restrict__ wg, const u16* __restrict__ wu,
                                               const u16* __restrict__ wd, float* __restrict__ ypart,
                                               int* __restrict__ idx_out, float* __restrict__ rsel_out) {
  __shared__ __attribute__((aligned(16))) u16 smem[3 * 8192 + 2 * (8 * 512 + 2 * 8 * 16 + 8 * 16 + 64)];
  int b = blockIdx.x;
  if (b < NMOE) {
    moe_block(b, r_in, xn, wg, wu, wd, ypart, idx_out, rsel_out, smem);
    return;
  }
  b -= NMOE;
  const int tid = threadIdx.x, wave = tid >> 6, lane = tid & 63;
  const int r = lane & 31, h = lane >> 5;
  const int head = (b & 7) * 4 + ((b >> 3) & 3);
  const int qb = b >> 5;
  const int qt = wave & 3, kh = wave >> 2;
  const int q0 = qb * 128 + qt * 32;

  const bf16x8 qf = *(const bf16x8*)(Q + ((size_t)(head * 2048 + q0 + r) * 16 + 8 * h));
  const int pir = (r & 0x13) | ((r & 4) << 1) | ((r & 8) >> 1);
  const u16* kp = K + ((size_t)(head * 2048 + kh * 1024 + pir) * 16 + 8 * h);
  const u16* vp = Vt + ((size_t)(head * 16 + (r & 15)) * 2048 + kh * 1024 + 8 * h);
  const short one = (short)0x3f80;
  const short vc = (r == 16) ? one : (short)0;
  const bf16x8 vconst = {vc, vc, vc, vc, vc, vc, vc, vc};

  constexpr float C = 0.25f * 1.4426950408889634f;
  float m = -INFINITY;
  f32x16 o;
#pragma unroll
  for (int i = 0; i < 16; ++i) o[i] = 0.f;
  f32x16 zero;
#pragma unroll
  for (int i = 0; i < 16; ++i) zero[i] = 0.f;

  bf16x8 k0, k1, v0 = vconst, v1 = vconst, v2 = vconst, v3 = vconst;
  k0 = *(const bf16x8*)(kp);
  k1 = *(const bf16x8*)(kp + 512);
  if (r < 16) {
    v0 = *(const bf16x8*)(vp);
    v1 = *(const bf16x8*)(vp + 16);
    v2 = *(const bf16x8*)(vp + 32);
    v3 = *(const bf16x8*)(vp + 48);
  }
  for (int kt = 0; kt < 16; ++kt) {
    const int kn = kt < 15 ? kt + 1 : 15;
    f32x16 s0 = MFMA32(k0, qf, zero);
    f32x16 s1 = MFMA32(k1, qf, zero);
    const bf16x8 c0 = v0, c1 = v1, c2 = v2, c3 = v3;
    k0 = *(const bf16x8*)(kp + (size_t)kn * 1024);
    k1 = *(const bf16x8*)(kp + (size_t)kn * 1024 + 512);
    if (r < 16) {
      v0 = *(const bf16x8*)(vp + kn * 64);
      v1 = *(const bf16x8*)(vp + kn * 64 + 16);
      v2 = *(const bf16x8*)(vp + kn * 64 + 32);
      v3 = *(const bf16x8*)(vp + kn * 64 + 48);
    }
    float t0, t1, t2, t3;
    __builtin_amdgcn_sched_barrier(0);
    asm volatile("s_nop 7\n\ts_nop 7" ::: "memory");
    __builtin_amdgcn_sched_barrier(0);
    t0 = max3(s0[0], s0[1], s0[2]);
    t1 = max3(s0[3], s0[4], s0[5]);
    t2 = max3(s1[0], s1[1], s1[2]);
    t3 = max3(s1[3], s1[4], s1[5]);
    t0 = max3(t0, s0[6], s0[7]);
    t1 = max3(t1, s0[8], s0[9]);
    t2 = max3(t2, s1[6], s1[7]);
    t3 = max3(t3, s1[8], s1[9]);
    t0 = max3(t0, s0[10], s0[11]);
    t1 = max3(t1, s0[12], s0[13]);
    t2 = max3(t2, s1[10], s1[11]);
    t3 = max3(t3, s1[12], s1[13]);
    t0 = max3(t0, s0[14], s0[15]);
    t2 = max3(t2, s1[14], s1[15]);
    t0 = max3(t0, t1, t2);
    float tmax = max3(t0, t3, t3);
    {
      auto rr = __builtin_amdgcn_permlane32_swap(__float_as_uint(tmax), __float_as_uint(tmax), false, false);
      tmax = max3(__uint_as_float(rr[0]), __uint_as_float(rr[1]), tmax);
    }
    if (__any(tmax > m)) {
      const float mn = fmaxf(m, tmax);
      const float alpha = __builtin_amdgcn_exp2f((m - mn) * C);
#pragma unroll
      for (int i = 0; i < 9; ++i) o[i] *= alpha;
      m = mn;
    }
    const float mc = -m * C;
#pragma unroll
    for (int i = 0; i < 16; ++i) {
      s0[i] = __builtin_amdgcn_exp2f(fmaf(s0[i], C, mc));
      s1[i] = __builtin_amdgcn_exp2f(fmaf(s1[i], C, mc));
    }
    o = MFMA32(c0, pack8(s0, 0), o);
    o = MFMA32(c1, pack8(s0, 1), o);
    o = MFMA32(c2, pack8(s1, 0), o);
    o = MFMA32(c3, pack8(s1, 1), o);
  }
  float* cb = (float*)smem;
  if (kh == 1) {
#pragma unroll
    for (int i = 0; i < 9; ++i) cb[(qt * 10 + i) * 64 + lane] = o[i];
    cb[(qt * 10 + 9) * 64 + lane] = m;
  }
  __syncthreads();
  if (kh == 0) {
    const float m2 = cb[(qt * 10 + 9) * 64 + lane];
    const float mn = fmaxf(m, m2);
    const float a1 = __builtin_amdgcn_exp2f((m - mn) * C), a2 = __builtin_amdgcn_exp2f((m2 - mn) * C);
    float of[9];
#pragma unroll
    for (int i = 0; i < 9; ++i) of[i] = o[i] * a1 + cb[(qt * 10 + i) * 64 + lane] * a2;
    const float l = __shfl(of[8], r);
    const float inv = 1.0f / l;
    u16* op = O + (size_t)(q0 + r) * 512 + head * 16 + 4 * h;
    u32x2 w0 = {pk2(of[0] * inv, of[1] * inv), pk2(of[2] * inv, of[3] * inv)};
    u32x2 w1 = {pk2(of[4] * inv, of[5] * inv), pk2(of[6] * inv, of[7] * inv)};
    *(u32x2*)(op) = w0;
    *(u32x2*)(op + 8) = w1;
  }
}

__global__ __launch_bounds__(256) void k3_out(const u16* __restrict__ O, const u16* __restrict__ wo,
                                              const u16* __restrict__ x, const u16* __restrict__ xn,
                                              const float* __restrict__ ypart, const int* __restrict__ idx_in,
                                              const float* __restrict__ rsel_in, u16* __restrict__ out) {
  __shared__ __attribute__((aligned(16))) u16 wl[512 * 32];
  const int tid = threadIdx.x, wave = tid >> 6, lane = tid & 63;
  const int r = lane & 31, h = lane >> 5;
  const int b = blockIdx.x;
  const int nt = (b & 7) | (((b >> 3) & 1) << 3);
  const int mt = b >> 4;
#pragma unroll
  for (int i = 0; i < 8; ++i) {
    const int piece = (i * 4 + wave) * 64 + lane;
    const int k = piece >> 2, c = (piece & 3) * 8;
    const u16* src = wo + (size_t)k * 512 + nt * 32 + c;
    __builtin_amdgcn_global_load_lds((const unsigned*)src, (unsigned*)(wl + (i * 4 + wave) * 512), 16, 0, 0);
  }
  const int row = mt * 128 + wave * 32 + r;
  const u16* orow = O + (size_t)row * 512 + 8 * h;
  u32x4 oa[32];
#pragma unroll
  for (int t = 0; t < 32; ++t) oa[t] = *(const u32x4*)(orow + 16 * t);
  asm volatile("s_waitcnt vmcnt(0)" ::: "memory");
  __syncthreads();
  const int g = lane >> 4, i16 = lane & 15;
  const u16* lb = wl + (8 * (g >> 1) + (i16 >> 2)) * 32 + 16 * (g & 1) + 4 * (i16 & 3);
  f32x16 acc;
#pragma unroll
  for (int i = 0; i < 16; ++i) acc[i] = 0.f;
#pragma unroll
  for (int t = 0; t < 32; ++t) {
    bf16x8 wf = tr_frag(lb, 16 * t * 32, 32);
    acc = MFMA32(wf, __builtin_bit_cast(bf16x8, oa[t]), acc);
  }
  int sel = -1;
#pragma unroll
  for (int j = 0; j < 8; ++j)
    if (idx_in[j] == row) sel = j;
  const float rs_sel = rsel_in[sel >= 0 ? sel : 0];
#pragma unroll
  for (int gq = 0; gq < 4; ++gq) {
    const int n = nt * 32 + 8 * gq + 4 * h;
    const size_t off = (size_t)row * 512 + n;
    const u32x2 xv = *(const u32x2*)(x + off);
    const u32x2 nv = *(const u32x2*)(xn + off);
    float xf[4] = {lo16(xv[0]), hi16(xv[0]), lo16(xv[1]), hi16(xv[1])};
    float nf[4] = {lo16(nv[0]), hi16(nv[0]), lo16(nv[1]), hi16(nv[1])};
    float res[4];
    const float4 ysum = *(const float4*)(ypart + (sel >= 0 ? sel : 0) * 512 + n);
    const float ys[4] = {ysum.x, ysum.y, ysum.z, ysum.w};
#pragma unroll
    for (int j = 0; j < 4; ++j) {
      const float upd = sel >= 0 ? rbf(rbf(ys[j]) * rs_sel) : 0.f;
      const float a = rbf(acc[4 * gq + j]);
      const float bb = rbf(nf[j] + upd);
      res[j] = rbf(xf[j] + a) + bb;
    }
    u32x2 w = {pk2(res[0], res[1]), pk2(res[2], res[3])};
    *(u32x2*)(out + off) = w;
  }
}

extern "C" void kernel_launch(void* const* d_in, const int* in_sizes, int n_in, void* d_out, int out_size,
                              void* d_ws, size_t ws_size, hipStream_t stream) {
  const u16* x = (const u16*)d_in[0];
  const u16* wqkv = (const u16*)d_in[1];
  const u16* wo = (const u16*)d_in[2];
  const u16* w_r = (const u16*)d_in[4];
  const u16* wg = (const u16*)d_in[5];
  const u16* wu = (const u16*)d_in[6];
  const u16* wd = (const u16*)d_in[7];
  char* ws = (char*)d_ws;
  const size_t MB2 = (size_t)2048 * 512 * 2;
  u16* xn = (u16*)(ws);
  u16* Q = (u16*)(ws + MB2);
  u16* K = (u16*)(ws + 2 * MB2);
  u16* Vt = (u16*)(ws + 3 * MB2);
  u16* O = (u16*)(ws + 4 * MB2);
  float* r = (float*)(ws + 5 * MB2);
  int* idx = (int*)(ws + 5 * MB2 + 8192);
  float* rsel = (float*)(ws + 5 * MB2 + 8192 + 64);
  float* ypart = (float*)(ws + 5 * MB2 + 16384);
  k0_ln<<<512, 256, 0, stream>>>(x, w_r, xn, r, ypart);
  k1_qkv<<<257, 256, 0, stream>>>(xn, wqkv, Q, K, Vt, r, idx, rsel);
  k2_attn<<<NMOE + 512, 512, 0, stream>>>(Q, K, Vt, O, r, xn, wg, wu, wd, ypart, idx, rsel);
  k3_out<<<256, 256, 0, stream>>>(O, wo, x, xn, ypart, idx, rsel, (u16*)d_out);
}
```
